```python
import torch

def _softmax(x, axis=-1):
    return torch.softmax(x, dim=axis)
def _gelu(x):
    return torch.nn.functional.gelu(x, approximate="tanh")
def _silu(x):
    return torch.nn.functional.silu(x)
def _sigmoid(x):
    return torch.sigmoid(x)
def _layernorm(x, axis=-1, eps=1e-5):
    m = x.mean(dim=axis, keepdim=True)
    v = x.var(dim=axis, keepdim=True, unbiased=False)
    return (x - m) / torch.sqrt(v + eps)
def _scatter_add(tgt, idx, src, axis=0):
    return tgt.clone().index_add_(axis, idx, src)
def _segment_sum(src, seg, n):
    out = torch.zeros((n,) + src.shape[1:], dtype=src.dtype)
    return out.index_add_(0, seg, src)
def _pad(x, pw):
    flat = tuple(v for pair in reversed(pw) for v in pair)
    return torch.nn.functional.pad(x, flat)

kernel_name = 'sequential_preLN__spectral_gfnet__glu_gated_glu__prompt'

def setup_inputs():

    _w = (lambda a: (lambda t: t.to(torch.bfloat16) if t.is_floating_point() else t)(torch.as_tensor(a)))
    def _A_setup():
        B, S, D = 1, 2048, 512
        Df = D // 2 + 1
        return {
            "x": _w((lambda *s: torch.randn(*s, dtype=torch.bfloat16) * 0.05)(B, S, D)),
            "w_r": _w((lambda *s: torch.randn(*s, dtype=torch.bfloat16) * 0.4)(S, Df) * 0.02),
            "w_i": _w((lambda *s: torch.randn(*s, dtype=torch.bfloat16) * 0.4)(S, Df) * 0.02),
        }
    _A_in = _A_setup()

    def _B_setup():
        B, S, D, F = 1, 2048, 512, 1792
        return {
            "x": _w((lambda *s: torch.randn(*s, dtype=torch.bfloat16) * 0.05)(B, S, D)),
            "w_gate": _w((lambda *s: torch.randn(*s, dtype=torch.bfloat16) * 0.05)(D, F)),
            "w_up": _w((lambda *s: torch.randn(*s, dtype=torch.bfloat16) * 0.05)(D, F)),
            "w_down": _w((lambda *s: torch.randn(*s, dtype=torch.bfloat16) * 0.05)(F, D)),
        }
    _B_in = _B_setup()

    return {
        "A_x": _A_in["x"],
        "A_w_r": _A_in["w_r"],
        "A_w_i": _A_in["w_i"],
        "B_x": _B_in["x"],
        "B_w_gate": _B_in["w_gate"],
        "B_w_up": _B_in["w_up"],
        "B_w_down": _B_in["w_down"],
    }

def reference(A_x, A_w_r, A_w_i, B_x, B_w_gate, B_w_up, B_w_down):
    def _A_ref(x, w_r, w_i):
        B, S, D = x.shape
        X = (lambda x, axes=(-2,-1): torch.fft.rfft2(x.float(), dim=axes))(x, axes=(-2, -1))
        W = w_r + 1j * w_i
        X = X * W
        y = (lambda x, s, axes=(-2,-1): torch.fft.irfft2(x, s=s, dim=axes).to(torch.bfloat16))(X, (S, D), axes=(-2, -1))
        return y

    def _B_ref(x, w_gate, w_up, w_down):
        g = torch.einsum("bsd,df->bsf", x, w_gate)
        u = torch.einsum("bsd,df->bsf", x, w_up)
        h = _sigmoid(g) * u
        return torch.einsum("bsf,fd->bsd", h, w_down)

    x = A_x
    h = x + _A_ref(_layernorm(x, axis=-1), A_w_r, A_w_i)
    h2 = h + _B_ref(_layernorm(h, axis=-1), B_w_gate, B_w_up, B_w_down)
    return h2
```

```cpp
#include <hip/hip_runtime.h>
#include <stdint.h>
#include <type_traits>

typedef unsigned short u16;
typedef unsigned int u32;
typedef unsigned long long u64;
typedef __attribute__((ext_vector_type(2))) __bf16 bf16x2;
typedef __attribute__((ext_vector_type(2))) float f32x2;
typedef __attribute__((ext_vector_type(4))) float f32x4;
typedef __attribute__((ext_vector_type(16))) float f32x16;
typedef __attribute__((ext_vector_type(2))) u32 u32x2;
typedef __attribute__((ext_vector_type(4))) u32 u32x4;
#define DI __device__ __forceinline__
#define GAS __attribute__((address_space(1)))
#define LAS __attribute__((address_space(3)))
#define RLX_AGENT __ATOMIC_RELAXED, __HIP_MEMORY_SCOPE_AGENT
#define SB() __builtin_amdgcn_sched_barrier(0)
#define ACLOB "a0","a1","a2","a3","a4","a5","a6","a7","a8","a9","a10","a11","a12","a13","a14","a15","a16","a17","a18","a19","a20","a21","a22","a23","a24","a25","a26","a27","a28","a29","a30","a31","a32","a33","a34","a35","a36","a37","a38","a39","a40","a41","a42","a43","a44","a45","a46","a47","a48","a49","a50","a51","a52","a53","a54","a55","a56","a57","a58","a59","a60","a61","a62","a63","a64","a65","a66","a67","a68","a69","a70","a71","a72","a73","a74","a75","a76","a77","a78","a79","a80","a81","a82","a83","a84","a85","a86","a87","a88","a89","a90","a91","a92","a93","a94","a95","a96","a97","a98","a99","a100","a101","a102","a103","a104","a105","a106","a107","a108","a109","a110","a111","a112","a113","a114","a115","a116","a117","a118","a119","a120","a121","a122","a123","a124","a125","a126","a127","a128","a129","a130","a131","a132","a133","a134","a135","a136","a137","a138","a139","a140","a141","a142","a143","a144","a145","a146","a147","a148","a149","a150","a151","a152","a153","a154","a155","a156","a157","a158","a159","a160","a161","a162","a163","a164","a165","a166","a167","a168","a169","a170","a171","a172","a173","a174","a175","a176","a177","a178","a179","a180","a181","a182","a183","a184","a185","a186","a187","a188","a189","a190","a191","a192","a193","a194","a195","a196","a197","a198","a199","a200","a201","a202","a203","a204","a205","a206","a207","a208","a209","a210","a211","a212","a213","a214","a215","a216","a217","a218","a219","a220","a221","a222","a223","a224","a225","a226","a227","a228","a229","a230","a231","a232","a233","a234","a235","a236","a237","a238","a239","a240","a241","a242","a243","a244","a245","a246","a247","a248","a249","a250","a251","a252","a253","a254","a255"

constexpr int S = 2048, D = 512, DF = 257, F = 1792;

constexpr int WS_Z   = 0;
constexpr int WS_WT  = WS_Z + (2 << 20);
constexpr int WS_Z2  = WS_WT + (3 << 20);
constexpr int WS_H   = WS_Z2 + (2 << 20);
constexpr int WS_ST  = WS_H + (2 << 20);
constexpr int WS_MU  = WS_ST + (512 << 10);
constexpr int WS_ACT = WS_ST + (1 << 20);
constexpr int WS_CTL = WS_ACT + (8 << 20);
constexpr int WS_W2  = WS_CTL + (2 << 20);
constexpr int WS_END = WS_W2 + (2 << 20);
constexpr u32 CODE_BYTES = 31616;
constexpr u32 NOWHERE = 0x40000000;
constexpr int FS = 2, FW = 16;
constexpr int LDS_BYTES = 163840;
constexpr int L_SCR = 131072;

DI u32 pack2(float lo, float hi) { return __builtin_bit_cast(u32, __builtin_convertvector((f32x2){lo, hi}, bf16x2)); }
DI float bflo(u32 v) { return __builtin_bit_cast(float, v << 16); }
DI float bfhi(u32 v) { return __builtin_bit_cast(float, v & 0xffff0000u); }
DI u32 f2u(float f) { return __builtin_bit_cast(u32, f); }
DI float u2f(u32 v) { return __builtin_bit_cast(float, v); }
DI float rbf(float f) { return bflo(pack2(f, 0.f)); }
template <class T> DI void pin(T& v) { asm volatile("" : "+v"(v)); }
template <class T, int N> DI void pin(T (&v)[N]) {
#pragma unroll
  for (int k = 0; k < N; ++k) pin(v[k]);
}
DI void pins(u32& v) { asm volatile("" : "+s"(v)); }
DI float cosr(float rev) { return __builtin_amdgcn_cosf(rev); }
DI float sinr(float rev) { return __builtin_amdgcn_sinf(rev); }
template <int CTRL> DI float dpp_add(float v) {
  return v + __builtin_bit_cast(float, __builtin_amdgcn_update_dpp(0, __builtin_bit_cast(int, v), CTRL, 0xf, 0xf, true));
}
DI float dot2(u32 a, u32 b, float c) { return __builtin_amdgcn_fdot2_f32_bf16(__builtin_bit_cast(bf16x2, a), __builtin_bit_cast(bf16x2, b), c, false); }
DI float sum8(float v) { v = dpp_add<0xB1>(v); v = dpp_add<0x4E>(v); v = dpp_add<0x141>(v); return v; }
DI void pair_lines(u32x4& a, u32x4& b) {
#pragma unroll
  for (int r = 0; r < 4; ++r) {
    const u32 x = a[r], y = b[r];
    a[r] = __builtin_amdgcn_update_dpp(x, y, 0x128, 0xf, 0xc, false);
    b[r] = __builtin_amdgcn_update_dpp(y, x, 0x128, 0xf, 0x3, false);
  }
}
DI float half_sum(float v) { auto sw = __builtin_amdgcn_permlane32_swap(f2u(v), f2u(v), false, false); return u2f(sw[0]) + u2f(sw[1]); }
DI float ln_sd(float var) { return rbf(__builtin_amdgcn_sqrtf(rbf(rbf(var) + 1e-5f))); }
DI float ln_rstd(float var) { return __builtin_amdgcn_rcpf(ln_sd(var)); }

DI void mm0(f32x4& c, const u32x4& a, const u32x4& b) { asm volatile("v_mfma_f32_16x16x32_bf16 %0, %1, %2, 0" : "=&v"(c) : "v"(a), "v"(b)); }
DI void mm(f32x4& c, const u32x4& a, const u32x4& b) { asm volatile("v_mfma_f32_16x16x32_bf16 %0, %1, %2, %0" : "+v"(c) : "v"(a), "v"(b)); }
template <int A> DI void mma0(f32x4& c, const u32x4& b) { asm volatile("v_mfma_f32_16x16x32_bf16 %0, a[%c2:%c3], %1, 0" : "=&v"(c) : "v"(b), "i"(A), "i"(A + 3)); }
template <int A> DI void mma(f32x4& c, const u32x4& b) { asm volatile("v_mfma_f32_16x16x32_bf16 %0, a[%c2:%c3], %1, %0" : "+v"(c) : "v"(b), "i"(A), "i"(A + 3)); }
DI void mw0(f32x16& c, const u32x4& a, const u32x4& b) { asm volatile("v_mfma_f32_32x32x16_bf16 %0, %1, %2, 0" : "=&v"(c) : "v"(a), "v"(b)); }
DI void mw(f32x16& c, const u32x4& a, const u32x4& b) { asm volatile("v_mfma_f32_32x32x16_bf16 %0, %1, %2, %0" : "+v"(c) : "v"(a), "v"(b)); }
#define NOP_IN2(a, b) asm volatile("s_nop 1" : "+v"(a), "+v"(b))
#define NOP_OUT4(a, b, c, d) asm volatile("s_nop 7" : "+v"(a), "+v"(b), "+v"(c), "+v"(d))

typedef u32x4 srd_t;
DI srd_t mk_srd(const void* p, u32 bytes) { const u64 a = (u64)p; return (srd_t){(u32)a, (u32)(a >> 32) & 0xffffu, bytes, 0x00020000u}; }
template <int N> DI void vmw() { asm volatile("s_waitcnt vmcnt(%0)" :: "i"(N > 63 ? 63 : N) : "memory"); }
template <int N> DI void vmw_bar() { asm volatile("s_waitcnt vmcnt(%0) lgkmcnt(0)\n\ts_barrier" :: "i"(N > 63 ? 63 : N) : "memory"); }
DI void bar() { asm volatile("s_waitcnt lgkmcnt(0)\n\ts_barrier" ::: "memory"); }
template <int IMM> DI void ld4(u32x4& d, u32 vo, srd_t r, u32 so) { asm volatile("buffer_load_dwordx4 %0, %1, %2, %3 offen offset:%4" : "=v"(d) : "v"(vo), "s"(r), "s"(so), "i"(IMM) : "memory"); }
template <int IMM> DI void ld4c(u32x4& d, u32 vo, srd_t r, u32 so) { asm volatile("buffer_load_dwordx4 %0, %1, %2, %3 offen offset:%4 sc1" : "=v"(d) : "v"(vo), "s"(r), "s"(so), "i"(IMM) : "memory"); }
template <int IMM> DI void ld2(u32x2& d, u32 vo, srd_t r, u32 so) { asm volatile("buffer_load_dwordx2 %0, %1, %2, %3 offen offset:%4" : "=v"(d) : "v"(vo), "s"(r), "s"(so), "i"(IMM) : "memory"); }
template <int IMM> DI void ld2c(u32x2& d, u32 vo, srd_t r, u32 so) { asm volatile("buffer_load_dwordx2 %0, %1, %2, %3 offen offset:%4 sc1" : "=v"(d) : "v"(vo), "s"(r), "s"(so), "i"(IMM) : "memory"); }
DI void ld1c(u32& d, u32 vo, srd_t r, u32 so) { asm volatile("buffer_load_dword %0, %1, %2, %3 offen sc1" : "=v"(d) : "v"(vo), "s"(r), "s"(so) : "memory"); }
template <int IMM> DI void ld1ci(u32& d, u32 vo, srd_t r, u32 so) { asm volatile("buffer_load_dword %0, %1, %2, %3 offen offset:%4 sc1" : "=v"(d) : "v"(vo), "s"(r), "s"(so), "i"(IMM) : "memory"); }
DI void ldh(u32& d, u32 vo, srd_t r, u32 so) { asm volatile("buffer_load_ushort %0, %1, %2, %3 offen" : "=v"(d) : "v"(vo), "s"(r), "s"(so) : "memory"); }
template <int A, int IMM> DI void ld4c_acc(u32 vo, srd_t r, u32 so) { asm volatile("buffer_load_dwordx4 a[%c3:%c4], %0, %1, %2 offen offset:%5 sc1" :: "v"(vo), "s"(r), "s"(so), "i"(A), "i"(A + 3), "i"(IMM) : "memory", ACLOB); }
DI void st4c(const u32x4& v, u32 vo, srd_t r, u32 so) { asm volatile("buffer_store_dwordx4 %0, %1, %2, %3 offen sc1\n\ts_nop 1" :: "v"(v), "v"(vo), "s"(r), "s"(so) : "memory"); }
DI void st1c(u32 v, u32 vo, srd_t r, u32 so) { asm volatile("buffer_store_dword %0, %1, %2, %3 offen sc1" :: "v"(v), "v"(vo), "s"(r), "s"(so) : "memory"); }
DI void st2c(const u32x2& v, u32 vo, srd_t r, u32 so) { asm volatile("buffer_store_dwordx2 %0, %1, %2, %3 offen sc1" :: "v"(v), "v"(vo), "s"(r), "s"(so) : "memory"); }
DI void touch(u32 vo, srd_t r) { asm volatile("buffer_load_dword a255, %0, %1, 0 offen" :: "v"(vo), "s"(r) : "memory", ACLOB); }
DI void dma(u32 lds, u32 vo, srd_t r, u32 so) { asm volatile("s_mov_b32 m0, %0\n\ts_nop 0\n\tbuffer_load_dwordx4 %1, %2, %3 offen lds" :: "s"(lds), "v"(vo), "s"(r), "s"(so) : "memory"); }
DI void dmac(u32 lds, u32 vo, srd_t r, u32 so) { asm volatile("s_mov_b32 m0, %0\n\ts_nop 0\n\tbuffer_load_dwordx4 %1, %2, %3 offen sc1 lds" :: "s"(lds), "v"(vo), "s"(r), "s"(so) : "memory"); }
template <int DL, int DS> DI void dmac_next(u32 vo, srd_t r, u32& so) {
  asm volatile("s_add_u32 m0, m0, %3\n\ts_add_u32 %0, %0, %4\n\tbuffer_load_dwordx4 %1, %2, %0 offen sc1 lds" : "+s"(so) : "v"(vo), "s"(r), "i"(DL), "i"(DS) : "memory", "scc");
}
struct W1Src { srd_t rg, ru; u32 vo, so, lds; };
constexpr int w1_slot(int ks) { constexpr int t[16] = {0, 1, 2, 3, 4, 5, 6, 7, 0, 1, 2, 7, 9, 3, 8, 9}; return t[ks] * 16384; }
template <int N> DI void w1_piece(const W1Src& s) {
  dma(s.lds + w1_slot(N >> 2) + (N & 1) * 8192 + (N & 2) * 512, s.vo, (N & 1) ? s.ru : s.rg, s.so + (32 * (N >> 2) + 2 * (N & 2)) * F * 2);
}
template <int I, int N, class Fn> DI void sfor(Fn&& f) { if constexpr (I < N) { f(std::integral_constant<int, I>{}); sfor<I + 1, N>(f); } }
template <int KS, int TAU, int T> DI void w1_tr1(u32 ad) {
  asm volatile("ds_read_b64_tr_b16 a[%c1:%c2], %0 offset:%c3" :: "v"(ad), "i"(16 * KS + 4 * TAU + 2 * T), "i"(16 * KS + 4 * TAU + 2 * T + 1),
               "i"((w1_slot(KS) & 0xffff) + T * 1024 + (TAU >> 1) * 8192) : "memory", ACLOB);
}
template <int KS, int HI> DI void w1_tr(u32 a0, u32 a1) {
  if constexpr (KS < HI) {
    const u32 b0 = a0 + (w1_slot(KS) & ~0xffff), b1 = a1 + (w1_slot(KS) & ~0xffff);
    w1_tr1<KS, 0, 0>(b0); w1_tr1<KS, 0, 1>(b0); w1_tr1<KS, 1, 0>(b1); w1_tr1<KS, 1, 1>(b1);
    w1_tr1<KS, 2, 0>(b0); w1_tr1<KS, 2, 1>(b0); w1_tr1<KS, 3, 0>(b1); w1_tr1<KS, 3, 1>(b1);
    w1_tr<KS + 1, HI>(a0, a1);
  }
}
DI void w1_tr_addr(int w, int l, u32& a0, u32& a1) {
  const int G = l >> 4, q = (l >> 2) & 3, p = l & 3, f = (q | ((G & 1) << 2)) << 1;
  const u32 row = (8 * G + q) * 256 + 8 * (p & 1);
  a0 = row + (((4 * w + (p >> 1)) ^ f) << 4); a1 = row + (((4 * w + 2 + (p >> 1)) ^ f) << 4);
}
template <int LO, int HI> DI void w1_tr_range(int w, int l) {
  u32 a0, a1;
  w1_tr_addr(w, l, a0, a1);
  w1_tr<LO, HI>(a0, a1);
}

DI void tr8x4(const u32x2 (&r)[8], u32x4 (&f)[4]) {
#pragma unroll
  for (int m = 0; m < 4; ++m) {
    f[0][m] = __builtin_amdgcn_perm(r[2 * m + 1][0], r[2 * m][0], 0x05040100u);
    f[1][m] = __builtin_amdgcn_perm(r[2 * m + 1][0], r[2 * m][0], 0x07060302u);
    f[2][m] = __builtin_amdgcn_perm(r[2 * m + 1][1], r[2 * m][1], 0x05040100u);
    f[3][m] = __builtin_amdgcn_perm(r[2 * m + 1][1], r[2 * m][1], 0x07060302u);
  }
}

template <int YOUNGER> DI void publish(GAS u64* flag, u64 tag) {
  vmw_bar<YOUNGER>();
  if (threadIdx.x == 0) __hip_atomic_store(flag, tag, RLX_AGENT);
  SB();
}
template <int N, int ST> DI void await(int w, GAS u64* flags, int idx, bool act, u64 tag) {
  if (w == 0) {
    for (int spins = 0; spins < (1 << 18); ++spins) {
      bool ok = true;
      if (act) {
#pragma unroll
        for (int j = 0; j < N; ++j) ok &= __hip_atomic_load(flags + (idx + 64 * j) * ST, RLX_AGENT) == tag;
      }
      if (__all(ok)) break;
    }
  }
  bar();
  SB();
}

struct Glu { float g0, g1; f32x2 sv, nx; u32x4 v; };
template <int STEP> DI void glu_step(Glu& e, f32x2 G, f32x2 U, int slot) {
  if constexpr (STEP == 0) { e.g0 = G[0] * e.sv[1]; pin(e.g0); SB(); e.g1 = G[1] * e.sv[1]; pin(e.g1); }
  if constexpr (STEP == 1) { e.g0 = __builtin_amdgcn_exp2f(e.g0); pin(e.g0); }
  if constexpr (STEP == 2) { e.g1 = __builtin_amdgcn_exp2f(e.g1); pin(e.g1); }
  if constexpr (STEP == 3) { e.g0 = e.g0 * e.sv[0] + e.sv[0]; pin(e.g0); SB(); e.g1 = e.g1 * e.sv[0] + e.sv[0]; pin(e.g1); }
  if constexpr (STEP == 4) { e.g0 = __builtin_amdgcn_rcpf(e.g0); pin(e.g0); }
  if constexpr (STEP == 5) { e.g1 = __builtin_amdgcn_rcpf(e.g1); pin(e.g1); }
  if constexpr (STEP == 6) { e.g0 *= U[0]; pin(e.g0); SB(); e.g1 *= U[1]; pin(e.g1); }
  if constexpr (STEP == 7) { u32 t = pack2(e.g0, e.g1); pin(t); e.v[slot] = t; }
}
DI f32x2 half2(const f32x4& v, int h) { return h ? (f32x2){v[2], v[3]} : (f32x2){v[0], v[1]}; }
DI u32x4 glu_tile(const f32x4 (&acc)[4], f32x2 sv) {
  f32x2 g[4];
  u32x4 v;
#pragma unroll
  for (int k = 0; k < 4; ++k) g[k] = half2(acc[k >> 1], k & 1) * sv[1];
#pragma unroll
  for (int k = 0; k < 4; ++k) g[k] = (f32x2){__builtin_amdgcn_exp2f(g[k][0]), __builtin_amdgcn_exp2f(g[k][1])} * sv[0] + sv[0];
#pragma unroll
  for (int k = 0; k < 4; ++k) { const f32x2 a = half2(acc[2 + (k >> 1)], k & 1) * (f32x2){__builtin_amdgcn_rcpf(g[k][0]), __builtin_amdgcn_rcpf(g[k][1])}; v[k] = pack2(a[0], a[1]); }
  return v;
}
constexpr int NV = 4;

template <int KS> DI void w2_load_acc(u32 vo, srd_t r, u32 so) {
  ld4c_acc<16 * (KS - NV), 0>(vo, r, so); ld4c_acc<16 * (KS - NV) + 4, 1024>(vo, r, so);
  ld4c_acc<16 * (KS - NV) + 8, 2048>(vo, r, so); ld4c_acc<16 * (KS - NV) + 12, 3072>(vo, r, so);
}
template <int LO, int HI> DI void w2_load_acc_range(u32 vo, srd_t r, u32 so) {
  if constexpr (LO < HI) { w2_load_acc<LO>(vo, r, so + (LO) * 4096); w2_load_acc_range<LO + 1, HI>(vo, r, so); }
}
DI void spin_until(u32x2 seen, u32x2 tag, GAS u64* fl) {
  if (__builtin_expect(__ballot(((seen[0] ^ tag[0]) | (seen[1] ^ tag[1])) != 0) != 0, 0)) {
    const u64 t = ((u64)tag[1] << 32) | tag[0];
    for (int spins = 0; spins < (1 << 18); ++spins) { if (__all(__hip_atomic_load(fl, RLX_AGENT) == t)) break; __builtin_amdgcn_s_sleep(2); }
  }
  SB();
}
DI void spin_one(u32x2 seen, u64 tag, GAS u64* fl) {
  const u64 v = ((u64)(u32)__builtin_amdgcn_readfirstlane(seen[1]) << 32) | (u32)__builtin_amdgcn_readfirstlane(seen[0]);
  if (__builtin_expect(v != tag, 0)) {
    for (int spins = 0; spins < (1 << 18); ++spins) { if (__all(__hip_atomic_load(fl, RLX_AGENT) == tag)) break; __builtin_amdgcn_s_sleep(2); }
  }
  SB();
}
struct G1 {
  f32x4 acc[2][4][4];
  u32x4 af[2][4], w2v[NV][4];
  Glu e;
  u32 sp[8];
  const LAS u32x4 *la, *lb;
  const LAS f32x2* lst;
  LAS f32x2* lsw;
  u32 vo, hs, hl, hn, w2s, as;
  srd_t rws;
  const W1Src* w1;
  u32 tr0, tr1, fvo, fso, f5o;
  u32x2 tag, pf, pg;
  u64 stag;
  GAS u64* f5;
};
template <int N> DI void h_piece(G1& c) { dmac(c.hl + N * 4096, c.vo, c.rws, c.hs + N * 4096); }
template <int P, int KS, int J> DI void g1_fill(G1& c) {
  if constexpr ((J & 1) && J < 8 && P * 16 + KS < 31) c.af[(KS + 1) & 1][J >> 1] = (P * 16 + KS < 15 ? c.la : c.lb)[(((KS + 1) & 15) * 4 + (J >> 1)) * 64];
  if constexpr (P == 0) {
    if constexpr (KS < 2 && (J == 2 || J == 8)) h_piece<8 + 2 * KS + (J >> 3)>(c);
    if constexpr (KS < 2 && (J == 5 || J == 12)) w1_piece<60 + 2 * KS + (J >> 3)>(*c.w1);
    if constexpr (KS == 1 && J == 14) { c.hn = c.hs + 11 * 4096; asm volatile("s_mov_b32 m0, %1" : "+s"(c.hn) : "s"(c.hl + 11 * 4096)); }
    if constexpr (KS >= 2 && KS < 12 && (J == 8 || J == 12)) asm volatile("s_add_u32 m0, m0, 0x1000\n\ts_add_u32 %0, %0, 0x1000" : "+s"(c.hn) :: "scc");
    if constexpr (KS >= 2 && KS < 12 && (J == 9 || J == 13)) asm volatile("buffer_load_dwordx4 %0, %1, %2 offen sc1 lds" :: "v"(c.vo), "s"(c.rws), "s"(c.hn) : "memory");
    if constexpr ((KS == 7 || KS == 8) && (J & 1) == 0) w1_tr1<7 + KS, (J >> 2), (J >> 1) & 1>((J & 4) ? c.tr1 : c.tr0);
    if constexpr (KS == 9 && J == 10) ld2c<0>(c.pf, 0, c.rws, c.f5o);
    if constexpr (KS == 12 && J == 2) { vmw<5>(); pin(c.pf); spin_one(c.pf, c.stag, c.f5); }
    if constexpr (KS == 12 && J == 4) ld2c<64>(c.pg, 0, c.rws, c.f5o);
    if constexpr (KS == 15 && J == 2) { pin(c.pg); spin_one(c.pg, c.stag, c.f5 + 8); }
    if constexpr (KS >= 12 && J >= 8 && (J & 1)) ld4c<((J - 8) >> 1) * 1024>(c.w2v[KS - 12][(J - 8) >> 1], c.vo, c.rws, c.w2s + (KS - 12) * 4096);
    if constexpr (KS == 4) {
      if constexpr (J == 2) { vmw<12>(); pin(c.sp); c.e.g1 = u2f(c.sp[0]) + u2f(c.sp[1]); pin(c.e.g1); }
      if constexpr (J == 4 || J == 6 || J == 10) { constexpr int k = J == 4 ? 2 : J == 6 ? 4 : 6; c.e.g1 += u2f(c.sp[k]) + u2f(c.sp[k + 1]); pin(c.e.g1); }
      if constexpr (J == 14) { c.e.g1 = half_sum(c.e.g1); pin(c.e.g1); }
    }
    if constexpr (KS == 5) {
      if constexpr (J == 2) { c.e.g1 = rbf(c.e.g1 * (1.0f / D)); pin(c.e.g1); }
      if constexpr (J == 4) { c.e.g1 = rbf(c.e.g1 + 1e-5f); pin(c.e.g1); }
      if constexpr (J == 6) { c.e.g1 = __builtin_amdgcn_sqrtf(c.e.g1); pin(c.e.g1); }
      if constexpr (J == 10) { c.e.g1 = rbf(c.e.g1); pin(c.e.g1); }
      if constexpr (J == 11) { c.e.g0 = __builtin_amdgcn_rcpf(c.e.g1); pin(c.e.g0); }
      if constexpr (J == 14) { float a = c.e.g1, r = c.e.g0 * -1.44269504089f; pin(a); pin(r); *c.lsw = (f32x2){a, r}; }
    }
    if constexpr (KS == 15 && J == 8) c.e.sv = c.lst[0];
  } else {
    constexpr int gp = J == 15 ? -1 : J < 8 ? ((J & 1) ? -1 : J >> 1) : J - 4;
    if constexpr (gp >= 0 && 11 * KS + gp < 128) {
      constexpr int st = 11 * KS + gp, rt = st >> 5, u = (st >> 3) & 3;
      if constexpr ((st & 31) == 0 && st > 0) c.e.sv = c.e.nx;
      glu_step<st & 7>(c.e, half2(c.acc[0][rt][u >> 1], u & 1), half2(c.acc[0][rt][2 + (u >> 1)], u & 1), u);
      if constexpr ((st & 31) == 15) c.e.nx = c.lst[16 * (rt + 1)];
      if constexpr ((st & 31) == 31) st4c(c.e.v, c.vo, c.rws, c.as + rt * 56 * 1024);
    }
    if constexpr (KS >= 1 && (J == 1 || J == 3 || J == 5)) {
      constexpr int n = 3 * (KS - 1) + (J >> 1);
      if constexpr (n < 4 * (14 - NV)) ld4c_acc<4 * n, (n & 3) * 1024>(c.vo, c.rws, c.w2s + (NV + (n >> 2)) * 4096);
    }
    if constexpr (KS == 15 && J == 0) { vmw<7>(); st2c(c.tag, c.fvo, c.rws, c.fso); }
  }
}
template <int P, int KS, int J> DI void g1_mm(G1& c) {
  if constexpr (J < 16) {
    if constexpr (J == 0) asm volatile("" : "+v"(c.af[KS & 1][0]), "+v"(c.af[KS & 1][1]), "+v"(c.af[KS & 1][2]), "+v"(c.af[KS & 1][3]));
    if constexpr (KS == 0) mma0<16 * KS + 4 * (J & 3)>(c.acc[P][J >> 2][J & 3], c.af[KS & 1][J >> 2]);
    else mma<16 * KS + 4 * (J & 3)>(c.acc[P][J >> 2][J & 3], c.af[KS & 1][J >> 2]);
    SB();
    g1_fill<P, KS, J>(c);
    SB();
    g1_mm<P, KS, J + 1>(c);
  }
}
template <int P, int KS, int HI> DI void g1_run(G1& c) {
  if constexpr (KS < HI) { g1_mm<P, KS, 0>(c); g1_run<P, KS + 1, HI>(c); }
}
constexpr int WIN = 10, NP = 19, NC = 23, NR = 36;
constexpr int g2_hi(int n) { return n < 0 ? WIN : n < NC ? (n + WIN + 1 < 28 ? n + WIN + 1 : 28) : (n + WIN + 1 < 56 ? n + WIN + 1 : 56); }
constexpr int g2_extra(int n) { return (n >= 0) + (n >= NP) + 3 * (n >= NR); }
constexpr int g2_step_of(int f) { int n = -1; while (g2_hi(n) <= f) ++n; return n; }
constexpr int g2_wait(int n) {
  const int m = g2_step_of(n);
  return g2_hi(n - 1) - 1 - n + g2_extra(n - 1) - (m < 0 ? 0 : g2_extra(m) - 3 * (m == NR));
}
struct G2 { float rr[16]; f32x4 acc[4][4]; u32x4 af[56], res0, res1; u32x2 pf, tag; u32 mu, vo, ab[4], fvo, fso, pvo, pso, rvo, rso, mvo; LAS f32x4* red[4]; GAS u64* fl; srd_t rws; };
template <int N> DI void g2_load(G2& c) { ld4c<0>(c.af[N], c.vo, c.rws, c.ab[N / 14] + (N % 14) * 1024); }
template <int N> DI void g2_run(G2& c, const u32x4 (&w2v)[NV][4]) {
  if constexpr (N < 56) {
    constexpr int ks = N % 14, r = N / 14;
    vmw<g2_wait(N)>();
    pin(c.af[N]);
    sfor<0, 4>([&](auto Q) {
      if constexpr (ks == 0) mm0(c.acc[r][Q], w2v[0][Q], c.af[N]);
      else if constexpr (ks < NV) mm(c.acc[r][Q], w2v[ks][Q], c.af[N]);
      else mma<16 * (ks - NV) + 4 * Q>(c.acc[r][Q], c.af[N]);
      if constexpr (Q == 0 && N == 0) st2c(c.tag, c.fvo, c.rws, c.fso);
      if constexpr (Q == 0 && N == NP) ld2c<0>(c.pf, c.pvo, c.rws, c.pso + 64 * FW * 8);
      if constexpr (Q == 0 && N == NC) { vmw<g2_hi(NC - 1) - g2_hi(NP - 1)>(); pin(c.pf); spin_until(c.pf, c.tag, c.fl + 64 * FW); }
      if constexpr (Q == 1) sfor<g2_hi(N - 1), g2_hi(N)>([&](auto Fr) { g2_load<Fr>(c); });
      if constexpr (Q == 3 && N == NR) { ld4c<0>(c.res0, c.rvo, c.rws, c.rso); ld4c<256>(c.res1, c.rvo, c.rws, c.rso); ld1c(c.mu, c.mvo, c.rws, WS_MU); }
      if constexpr (N >= 48 && (Q & 1)) {
        constexpr int e = 2 * (N - 48) + (Q >> 1);
        if constexpr (e == 0) { pin(c.res0); pin(c.res1); pin(c.mu); }
        const u32 v = e < 8 ? c.res0[(e >> 1) & 3] : c.res1[(e >> 1) & 3];
        float t = ((e & 1) ? bfhi(v) : bflo(v)) + u2f(c.mu);
        pin(t);
        c.rr[e] = t;
      }
      if constexpr (Q == 2 && r > 0 && ks >= 3 && ks < 11 && (ks & 1)) { pin(c.acc[r - 1][(ks - 3) >> 1]); c.red[r - 1][((ks - 3) >> 1) * 64] = c.acc[r - 1][(ks - 3) >> 1]; }
      SB();
    });
    g2_run<N + 1>(c, w2v);
  }
}
template <int KS> DI void p3_run(f32x16& acc, const u32x4 (&ta)[16], u32x4 (&z)[3], const LAS u32* zi, const W1Src& w1, float (&xc)[16], u32& mu) {
  if constexpr (KS < 16) {
    if constexpr (KS + 2 < 16) {
      if constexpr (KS == 4) { vmw_bar<4>(); pin(mu); }
      z[(KS + 2) % 3] = (u32x4){zi[(KS + 2) * 256], zi[(KS + 2) * 256 + 32], zi[(KS + 2) * 256 + 64], zi[(KS + 2) * 256 + 96]};
    }
    SB();
    if constexpr (KS == 0) mw0(acc, ta[KS], z[KS % 3]); else mw(acc, ta[KS], z[KS % 3]);
    if constexpr (KS < 8) w1_piece<44 + KS>(w1);
    if constexpr (KS >= 8) { xc[2 * KS - 16] -= u2f(mu); pin(xc[2 * KS - 16]); xc[2 * KS - 15] -= u2f(mu); pin(xc[2 * KS - 15]); }
    SB();
    p3_run<KS + 1>(acc, ta, z, zi, w1, xc, mu);
  }
}

DI void body(const u16* __restrict__ x, const u16* __restrict__ wr, const u16* __restrict__ wi, const u16* __restrict__ wg,
             const u16* __restrict__ wu, const u16* __restrict__ wd, unsigned char* __restrict__ ws, u16* __restrict__ out) {
  extern __shared__ __attribute__((aligned(16))) unsigned char smem_[];
  LAS unsigned char* smem = (LAS unsigned char*)smem_;
  const int b = blockIdx.x, tid = threadIdx.x;
  const int w = __builtin_amdgcn_readfirstlane(tid >> 6), l = tid & 63, i = l & 15, g = l >> 4;
  const int xcd = b & 7, idx = b >> 3;
  const srd_t rws = mk_srd(ws, WS_END), rx = mk_srd(x, S * D * 2);
  GAS u64* ctl = (GAS u64*)(ws + WS_CTL);
  GAS u64* F1 = ctl + 32768, *F2 = ctl + 2 * 32768, *F3 = ctl + 3 * 32768, *F4 = ctl + 4 * 32768, *F5 = ctl + 5 * 32768;
  const int rb = xcd * 8 + (idx >> 2), rl = tid >> 3, c8 = l & 7;
  const int srow1 = 32 * (4 * (rb >> 2) + (rl & 3)) + 16 * (rb & 1) + 8 * ((rb >> 1) & 1) + (rl >> 2);
  u32x4 xv[8];
  {
    u32 vo = srow1 * (D * 2) + 16 * c8;
    pin(vo);
    asm volatile("" :: "s"(x), "s"(wr), "s"(wi), "s"(wg), "s"(wu), "s"(wd), "s"(ws), "s"(out));
    ld4<0>(xv[0], vo, rx, 0); ld4<128>(xv[1], vo, rx, 0); ld4<256>(xv[2], vo, rx, 0); ld4<384>(xv[3], vo, rx, 0);
    ld4<512>(xv[4], vo, rx, 0); ld4<640>(xv[5], vo, rx, 0); ld4<768>(xv[6], vo, rx, 0); ld4<896>(xv[7], vo, rx, 0);
    SB();
  }
  const bool ffn = b < 224;
  const int late = (23 - idx) >> 31;
  const int m128 = 8 * (xcd & 1) + ((idx & 7 & ~late) | (((idx & 3) + 2 * (xcd & 2)) & late)), ch = 7 * (xcd >> 2) + (((2 * (xcd & 2) + (idx >> 3)) & ~late) | (3 & late));
  const int m64 = 4 * xcd + (idx >> 3), cb = idx & 7;
  W1Src w1;
  w1.rg = mk_srd(wg, ffn ? D * F * 2 : 0); w1.ru = mk_srd(wu, ffn ? D * F * 2 : 0);
  w1.vo = g * F * 2 + ((i ^ ((g | ((w & 1) << 2)) << 1)) << 4); w1.so = (8 * w * F + 128 * ch) * 2; w1.lds = w * 2048;
  u64 tag;
  GAS u64* f1p;

  {
    LAS u32x4* lds = (LAS u32x4*)(smem + L_SCR + 16384);
    const int c = c8, srow = srow1, q = idx & 3, par = q & 1, range = q >> 1;
    const srd_t rr = mk_srd(wr, S * DF * 2), ri = mk_srd(wi, S * DF * 2);
    const int krow = 16 * (rb & 3) + 8 * ((rb >> 2) & 1) + 256 * (rb >> 3);
    u32 c256[2][4] = {};
    const bool nyq = q == 0 && w == 0;
    const int kd = 2 * (64 * range + 16 * w + i) + par;
    u32x4 bre[8], bim[8];
    u32x2 ev;
    {
      float st = kd ? (float)kd * (1.0f / 512.0f) : 0.5f;
      pin(st);
      const float a8 = (float)(8 * g) * st;
      const u32 zm = kd ? 0xffffffffu : 0u;
      const u32 sq = (kd & 2) ? 0x80008000u : 0u, sp = par ? sq ^ 0x80008000u : sq;
      sfor<0, 4>([&](auto KS) {
        constexpr int ks = KS;
#pragma unroll
        for (int jp = 0; jp < 4; ++jp) {
          const float a0 = __builtin_fmaf(st, (float)(32 * ks + 2 * jp), a8), a1 = __builtin_fmaf(st, (float)(32 * ks + 2 * jp + 1), a8);
          bre[ks][jp] = pack2(cosr(a0), cosr(a1));
          bim[ks][jp] = pack2(sinr(-a0), sinr(-a1)) & zm;
          bre[ks + 4][jp] = (par ? bim[ks][jp] : bre[ks][jp]) ^ sq;
          bim[ks + 4][jp] = (par ? bre[ks][jp] : bim[ks][jp]) ^ sp;
        }
        pin(bre[ks]); pin(bim[ks]); pin(bre[ks + 4]); pin(bim[ks + 4]);
        if constexpr (ks == 2) {
#pragma unroll
          for (int n = 0; n < 2; ++n) {
            const int ip = 4 * n + g;
            const u32 vo = (((krow + 64 * w + ip) * DF + 128 * range + par) * 2 & ~3) + ((i ^ ip) << 4);
            dma(L_SCR + (8 * w + 4 * n) * 256, vo, rr, 0);
            dma(L_SCR + 8192 + (8 * w + 4 * n) * 256, vo, ri, 0);
          }
          ld2c<0>(ev, 0, rws, WS_CTL);
          if (nyq) {
#pragma unroll
            for (int r = 0; r < 4; ++r) { ldh(c256[0][r], ((l & 7) * DF + 256) * 2, rr, (krow + 64 * r) * DF * 2); ldh(c256[1][r], ((l & 7) * DF + 256) * 2, ri, (krow + 64 * r) * DF * 2); }
          }
        }
      });
    }
    LAS u32x4* lw0 = lds + (rl >> 4) * 512 + (c >> 2) * 64 + (c & 3) * 16 + ((rl & 15) ^ ((c & 3) | ((c >> 2) ? 12 : 0)));
    const LAS u32x4* lr0 = lds + g * 16 + (i ^ g), * lr1 = lds + 64 + g * 16 + (i ^ (g | 12));
    const LAS unsigned char* wp0 = smem + L_SCR + (l & 7) * 256 + ((((16 * w + (l >> 3)) >> 2) ^ (l & 7)) << 4) + (((l >> 3) & 3) << 2) + (((l + par) & 1) << 1);
    const LAS unsigned char* wp1 = smem + L_SCR + (l & 7) * 256 + ((((16 * w + 8 + (l >> 3)) >> 2) ^ (l & 7)) << 4) + (((l >> 3) & 3) << 2) + (((l + par) & 1) << 1);
    u32 wvo = ((2 * (64 * range + 16 * w + (l >> 3)) + par) * 512 + (l & 7)) * 16, zvo = ((kd - 2 * (i & 8)) * 512 + (i >> 3) * 4 + g) * 16;
    u32 mvo = (q == 0 && c == 0) ? srow * 4 : NOWHERE;
    u32 wso0 = WS_WT + ((rb & 3) * 128 + (rb >> 3) * 16 + 8 * ((rb >> 2) & 1)) * 16, wso1 = wso0 + 16 * 8192;
    u32 zso0 = WS_Z + ((rb & 1) * 256 + (rb >> 2) * 16 + 8 * ((rb >> 1) & 1)) * 16, zso1 = zso0 + 16 * 8192;
    pins(wso0); pins(wso1); pins(zso0); pins(zso1); pins(w1.so); pins(w1.lds);
    asm volatile("" : "+s"(w1.rg), "+s"(w1.ru));
    pin(lw0); pin(lr0); pin(lr1); pin(wp0); pin(wp1); pin(wvo); pin(zvo); pin(mvo); pin(w1.vo);
    asm volatile("s_cmp_eq_u32 %0, 0\n\ts_cbranch_scc1 1f\n\ts_waitcnt vmcnt(5)\n1:\n\ts_waitcnt vmcnt(13)" :: "s"(q | w) : "memory", "scc");
    pin(xv);
    {
      const srd_t rg = mk_srd((w & 1) ? wu : wg, w < 2 ? D * F * 2 : 0);
      touch((b * 56 + l) * 128 + (l < 56 ? 0 : NOWHERE), rg);
    }
    float sm[4] = {}, sq[4] = {};
    sfor<0, 8>([&](auto J) {
#pragma unroll
      for (int e = 0; e < 4; ++e) { sm[e] = dot2(xv[J][e], 0x3f803f80u, sm[e]); sq[e] = dot2(xv[J][e], xv[J][e], sq[e]); }
      pin(sm); pin(sq);
    });
    const float mean = sum8((sm[0] + sm[1]) + (sm[2] + sm[3])) * (1.0f / D);
    const float var = __builtin_fmaxf(sum8((sq[0] + sq[1]) + (sq[2] + sq[3])) * (1.0f / D) - mean * mean, 0.0f);
    f32x2 xs[16];
    {
      const u32 pm = par ? 0xbf803f80u : 0x3f803f80u;
      sfor<0, 4>([&](auto J) {
#pragma unroll
        for (int e = 0; e < 4; ++e) {
          xs[4 * J + e] = (f32x2){dot2(__builtin_amdgcn_perm(xv[J + 4][e], xv[J][e], 0x05040100u), pm, 0.f), dot2(__builtin_amdgcn_perm(xv[J + 4][e], xv[J][e], 0x07060302u), pm, 0.f)};
          pin(xs[4 * J + e]);
        }
      });
    }
    const float rs = ln_rstd(var) * (1.0f / 524288.0f), off = par ? 0.0f : -2.0f * mean * rs;
#pragma unroll
    for (int j = 0; j < 4; ++j) {
      u32x4 v;
#pragma unroll
      for (int e = 0; e < 4; ++e) { const f32x2 a = xs[4 * j + e] * rs + off; v[e] = pack2(a[0], a[1]); }
      lw0[j * 128] = v;
    }
    vmw_bar<1>();
    u32x4 af[16];
#pragma unroll
    for (int ks = 0; ks < 8; ++ks)
#pragma unroll
      for (int t = 0; t < 2; ++t) af[2 * ks + t] = ((ks & 1) ? lr1 : lr0)[t * 512 + (ks >> 1) * 128];
    SB();
    st1c(f2u(mean), mvo, rws, WS_MU);
    u32 cn[4], nm = (nyq && l < 8) ? 0xffffffffu : 0u;
    pin(c256[0]); pin(c256[1]); pin(nm);
#pragma unroll
    for (int r = 0; r < 4; ++r) { cn[r] = __builtin_amdgcn_perm(c256[1][r], c256[0][r], 0x05040100u) & nm; pin(cn[r]); }
    pin(ev);
    tag = (((u64)ev[1] << 32) | ev[0]) + 1;
    SB();
    u32 wt[2][2][4];
    f32x4 are[2], aim[2];
    sfor<0, 32>([&](auto N) {
      constexpr int ks = N >> 2, t = (N >> 1) & 1;
      if constexpr (N & 1) { if constexpr (ks == 0) mm0(aim[t], af[2 * ks + t], bim[ks]); else mm(aim[t], af[2 * ks + t], bim[ks]); }
      else { if constexpr (ks == 0) mm0(are[t], af[2 * ks + t], bre[ks]); else mm(are[t], af[2 * ks + t], bre[ks]); }
      if constexpr (N >= 4 && N < 20) {
        constexpr int k = N - 4;
        wt[k >> 3][k & 1][(k >> 1) & 3] = *(const LAS u16*)(((k >> 3) ? wp1 : wp0) + ((k >> 1) & 3) * 2048 + (k & 1) * 8192);
      }
      if constexpr (N == 22 || N == 27) {
        constexpr int z = N == 27;
        u32x4 v;
#pragma unroll
        for (int r = 0; r < 4; ++r) {
          v[r] = __builtin_amdgcn_perm(wt[z][1][r], wt[z][0][r], 0x05040100u);
          if constexpr (z == 0) v[r] = (v[r] & ~nm) | cn[r];
        }
        st4c(v, wvo, rws, z ? wso1 : wso0);
      }
      SB();
    });
    NOP_OUT4(are[0], are[1], aim[0], aim[1]);
    {
      u32x4 v0, v1;
#pragma unroll
      for (int r = 0; r < 4; ++r) { v0[r] = pack2(are[0][r], aim[0][r]); v1[r] = pack2(are[1][r], aim[1][r]); }
      pair_lines(v0, v1);
      st4c(v0, zvo, rws, zso0);
      st4c(v1, zvo, rws, zso1);
    }
    f1p = F1 + (q * 64 + rb) * FS;
  }

  {
    LAS u32x4* lds = (LAS u32x4*)(smem + L_SCR);
    u32x4 f1re[4], f1im[4], f2c[2], f2s[2], f2n[2];
    f32x4 t1c[2], t1s[2], t2c[2], t2s[2];
    {
      const int k1 = 16 * w + i;
      const float st = (float)k1 * (1.0f / 64.0f), a4 = (float)(4 * g) * st;
      const u32 sh = (k1 & 1) << 4, h2 = (k1 & 1) ? 0x80008000u : 0u;
      const u32 q1 = (k1 & 2) ? ((k1 & 1) ? 0x80000000u : 0x80008000u) : ((k1 & 1) ? 0x00008000u : 0u);
      sfor<0, 4>([&](auto J_) {
        constexpr int J = J_;
        const float a = __builtin_fmaf(st, (float)J, a4);
        const float cs = cosr(a), sn = sinr(a);
        u32 p = pack2(cs, sn), q = pack2(-sn, cs);
        u32 p1 = __builtin_amdgcn_alignbit(p, p, sh) ^ q1, r1 = __builtin_amdgcn_alignbit(q, q, sh) ^ q1;
        pin(p1); pin(r1);
        f1re[0][J] = p; f1im[0][J] = q; f1re[1][J] = p1; f1im[1][J] = r1;
        f1re[2][J] = p ^ h2; f1im[2][J] = q ^ h2; f1re[3][J] = p1 ^ h2; f1im[3][J] = r1 ^ h2;
        w1_piece<J>(w1);
      });
    }
    {
      const float st = (float)i * (1.0f / 32.0f), a4 = (float)(4 * g) * st;
      const u32 hi = (i & 1) ? 0x80008000u : 0u;
#pragma unroll
      for (int j = 0; j < 2; ++j) {
        const float a0 = __builtin_fmaf(st, (float)(2 * j), a4), a1 = __builtin_fmaf(st, (float)(2 * j + 1), a4);
        const u32 p = pack2(cosr(a0), cosr(a1)), q = pack2(sinr(a0), sinr(a1));
        f2c[0][j] = p; f2c[0][j + 2] = p ^ hi; f2s[0][j] = q; f2s[0][j + 2] = q ^ hi;
      }
      f2c[1] = f2c[0] ^ 0x80000000u; f2s[1] = f2s[0] ^ 0x80000000u; f2n[0] = f2s[0] ^ 0x80008000u; f2n[1] = f2s[0] ^ 0x00008000u;
    }
    {
      const float s1 = (float)(16 * w + i) * (1.0f / 2048.0f), b1 = (float)(4 * g) * s1;
#pragma unroll
      for (int t = 0; t < 2; ++t)
#pragma unroll
        for (int r = 0; r < 4; ++r) { const float a1 = __builtin_fmaf(s1, (float)(16 * t + r), b1); t1c[t][r] = cosr(a1); t1s[t][r] = sinr(a1); }
    }
    pin(f1re); pin(f1im); pin(f2c); pin(f2s); pin(f2n); pin(t1c); pin(t1s);
    publish<4>(f1p, tag);
    sfor<0, 8>([&](auto I) {
      constexpr int t = I >> 2, r = I & 3;
      w1_piece<4 + I + (I >> 1)>(w1);
      if constexpr (I & 1) w1_piece<5 + I + (I >> 1)>(w1);
      const float s2 = (float)(16 * t + i) * (1.0f / 2048.0f), a2 = __builtin_fmaf(s2, (float)r, (float)(16 * w + 4 * g) * s2);
      float c2 = cosr(a2), n2 = sinr(-a2);
      pin(c2); pin(n2);
      t2c[t][r] = c2; t2s[t][r] = n2;
    });
    LAS u32x4* lp = lds + l;
    u32 z2o = (32 * (16 * w + (i & 7)) + 16 * (i >> 3) + 4 * g) * 4;
    pin(t2c); pin(t2s); pin(lp); pin(z2o);
    const int col = b;
    await<1, FS>(w, F1, ((col & 1) | ((col >> 7) << 1)) * 64 + l, true, tag);
    u32x4 zf[8], wv[2];
    dmac(L_SCR + 8192 + w * 2048, l * 16, rws, WS_Z + col * 8192 + w * 2048);
    dmac(L_SCR + 8192 + w * 2048 + 1024, l * 16, rws, WS_Z + col * 8192 + w * 2048 + 1024);
    ld4c<0>(wv[0], l * 16, rws, WS_WT + (col * 4 + w) * 2048); ld4c<1024>(wv[1], l * 16, rws, WS_WT + (col * 4 + w) * 2048);
    vmw_bar<2>();
#pragma unroll
    for (int k = 0; k < 8; ++k) zf[k] = lp[(8 + (k & 1) * 4 + (k >> 1)) * 64];
    SB();
    f32x4 tr[2], ti[2];
    mm0(tr[0], zf[0], f1re[0]); mm0(ti[0], zf[0], f1im[0]); mm0(tr[1], zf[1], f1re[0]); mm0(ti[1], zf[1], f1im[0]);
    w1_piece<16>(w1);
    mm(tr[0], zf[2], f1re[1]); mm(ti[0], zf[2], f1im[1]); mm(tr[1], zf[3], f1re[1]); mm(ti[1], zf[3], f1im[1]);
    w1_piece<17>(w1);
    mm(tr[0], zf[4], f1re[2]); mm(ti[0], zf[4], f1im[2]); mm(tr[1], zf[5], f1re[2]); mm(ti[1], zf[5], f1im[2]);
    w1_piece<18>(w1);
    mm(tr[0], zf[6], f1re[3]); mm(ti[0], zf[6], f1im[3]); mm(tr[1], zf[7], f1re[3]); mm(ti[1], zf[7], f1im[3]);
    w1_piece<19>(w1);
    vmw<4>(); pin(wv);
    NOP_OUT4(tr[0], ti[0], tr[1], ti[1]);
    u32x4 br, bi;
#pragma unroll
    for (int t = 0; t < 2; ++t) {
      const f32x4 pr = tr[t] * t1c[t] + ti[t] * t1s[t], pi = ti[t] * t1c[t] - tr[t] * t1s[t];
      br[2 * t] = pack2(pr[0], pr[1]); br[2 * t + 1] = pack2(pr[2], pr[3]); bi[2 * t] = pack2(pi[0], pi[1]); bi[2 * t + 1] = pack2(pi[2], pi[3]);
    }
    NOP_IN2(br, bi);
    f32x4 xr[2], xi[2];
    mm0(xr[0], f2c[0], br); mm0(xi[0], f2c[0], bi); mm0(xr[1], f2c[1], br); mm0(xi[1], f2c[1], bi);
    w1_piece<20>(w1);
    w1_piece<21>(w1);
    mm(xr[0], f2s[0], bi); mm(xi[0], f2n[0], br); mm(xr[1], f2s[1], bi); mm(xi[1], f2n[1], br);
    w1_piece<22>(w1);
    w1_piece<23>(w1);
    f32x4 wa[2], wb[2];
#pragma unroll
    for (int t = 0; t < 2; ++t)
#pragma unroll
      for (int r = 0; r < 4; ++r) { wa[t][r] = bflo(wv[t][r]); wb[t][r] = bfhi(wv[t][r]); }
    pin(wa); pin(wb);
    NOP_OUT4(xr[0], xi[0], xr[1], xi[1]);
    u32x4 yr, yi;
#pragma unroll
    for (int t = 0; t < 2; ++t) {
      const f32x4 pr = xr[t] * wa[t] - xi[t] * wb[t], pi = xr[t] * wb[t] + xi[t] * wa[t];
      yr[2 * t] = pack2(pr[0], pr[1]); yr[2 * t + 1] = pack2(pr[2], pr[3]); yi[2 * t] = pack2(pi[0], pi[1]); yi[2 * t + 1] = pack2(pi[2], pi[3]);
    }
    NOP_IN2(yr, yi);
    f32x4 ur[2], ui[2];
    mm0(ur[0], yr, f2c[0]); mm0(ui[0], yr, f2s[0]); mm0(ur[1], yr, f2c[1]); mm0(ui[1], yr, f2s[1]);
    w1_piece<24>(w1);
    w1_piece<25>(w1);
    mm(ur[0], yi, f2n[0]); mm(ui[0], yi, f2c[0]); mm(ur[1], yi, f2n[1]); mm(ui[1], yi, f2c[1]);
    w1_piece<26>(w1);
    w1_piece<27>(w1);
    NOP_OUT4(ur[0], ui[0], ur[1], ui[1]);
#pragma unroll
    for (int t = 0; t < 2; ++t) {
      const f32x4 a = ur[t] * t2c[t] + ui[t] * t2s[t], nb = ur[t] * t2s[t] - ui[t] * t2c[t];
      lp[(t * 4 + w) * 64] = (u32x4){pack2(a[0], nb[0]), pack2(a[1], nb[1]), pack2(a[2], nb[2]), pack2(a[3], nb[3])};
    }
    bar();
    u32x4 uf[8];
#pragma unroll
    for (int k = 0; k < 8; ++k) uf[k] = lp[((k & 1) * 4 + (k >> 1)) * 64];
    SB();
    f32x4 or_[2], oi[2];
    mm0(or_[0], uf[0], f1re[0]); mm0(oi[0], uf[0], f1im[0]); mm0(or_[1], uf[1], f1re[0]); mm0(oi[1], uf[1], f1im[0]);
#pragma unroll
    for (int ks = 1; ks < 4; ++ks) { mm(or_[0], uf[2 * ks], f1re[ks]); mm(oi[0], uf[2 * ks], f1im[ks]); mm(or_[1], uf[2 * ks + 1], f1re[ks]); mm(oi[1], uf[2 * ks + 1], f1im[ks]); }
    NOP_OUT4(or_[0], oi[0], or_[1], oi[1]);
    {
      u32x4 v0, v1;
#pragma unroll
      for (int r = 0; r < 4; ++r) { v0[r] = pack2(or_[0][r], oi[0][r]); v1[r] = pack2(or_[1][r], oi[1][r]); }
      pair_lines(v0, v1);
      st4c(v0, z2o, rws, WS_Z2 + col * 8192);
      st4c(v1, z2o, rws, WS_Z2 + col * 8192 + 1024);
    }
  }

  G1 c;
  {
    LAS f32x4* lds = (LAS f32x4*)(smem + L_SCR);
    const int sl = l & 31, h = l >> 5, par = w & 1;
    const int m32 = 8 * xcd + (idx >> 2), cd = 2 * (idx & 3) + (w >> 1);
    const int s = 32 * m32 + sl;
    const int d0 = 32 * cd + 16 * h + 8 * par;
    u32x4 x0, x1;
    w1_tr_range<0, 4>(w, l);
    u32x4 ta[16];
    {
      const int dl = 32 * cd + 16 * ((sl >> 2) & 1) + ((4 * (sl >> 3) + (sl & 3)) ^ (8 * par));
      const float st = (float)dl * (1.0f / 512.0f);
      const u32 sh = (dl & 1) << 4, sx = (dl & 2) ? ((dl & 1) ? 0x80000000u : 0x80008000u) : ((dl & 1) ? 0x00008000u : 0u);
      const float a8 = (float)(8 * h + par) * st;
      const u32 nyqv = (dl & 1) ? 0xbf00u : 0x3f00u;
      sfor<0, 32>([&](auto I) {
        constexpr int ks = I >> 2, j = I & 3;
        const float a = __builtin_fmaf(st, (float)(16 * ks + 2 * j), a8);
        u32 v = pack2(cosr(a), sinr(a)), v2 = __builtin_amdgcn_alignbit(v, v, sh) ^ sx;
        if constexpr (I == 0) v = (h == 0 && par == 0) ? nyqv : v;
        pin(v); pin(v2);
        ta[2 * ks][j] = v; ta[2 * ks + 1][j] = v2;
        if constexpr (I < 8 && (I & 1)) w1_piece<28 + I / 2>(w1);
        if constexpr (I == 11) {
          publish<4>(F2 + b * FS, tag);
          touch((b * 56 + l) * 128 + (l < 56 ? 0 : NOWHERE), mk_srd(wd, w == 2 ? D * F * 2 : 0));
          w1_tr_range<4, 7>(w, l);
          ld4<0>(x0, (s * D + d0) * 2, rx, 0); ld4<512>(x1, (s * D + d0) * 2, rx, 0);
        }
        if constexpr (I >= 12 && I < 24) w1_piece<20 + I>(w1);
      });
    }
    pin(ta);
    u32 zvo = ((2 * (l >> 3) + par + 128 * (w >> 1)) * S + 32 * m32) * 4 + (l & 7) * 16;
    u32 ho = (((s >> 4) & 3) * 64 + ((d0 >> 3) & 3) * 16 + (s & 15)) * 16, sto = h ? NOWHERE : (s & 127) * 4;
    const u32 zl = (3 + par) * 16384;
    const LAS u32* zi = (const LAS u32*)(smem + zl) + h * 128 + sl;
    LAS f32x4* xw = lds + w * 128 + l;
    const LAS f32x4* xr = lds + (w ^ 1) * 128 + l;
    pin(zvo); pin(ho); pin(sto); pin(zi); pin(xw); pin(xr);
    vmw_bar<12>();
    w1_tr_range<7, 8>(w, l);
    pin(x0); pin(x1);
    float xc[16];
#pragma unroll
    for (int e = 0; e < 4; ++e) { xc[2 * e] = bflo(x0[e]); xc[2 * e + 1] = bfhi(x0[e]); xc[8 + 2 * e] = bflo(x1[e]); xc[9 + 2 * e] = bfhi(x1[e]); }
    u32 muo = s * 4, zl0 = zl + (w >> 1) * 1024, zso = WS_Z2;
    pin(xc); pin(muo); pins(zl0); pins(zso);
    await<4, FS>(w, F2, l, true, tag);
    u32 mu;
    dmac(zl0, zvo, rws, zso);
    sfor<1, 8>([&](auto K) { dmac_next<2048, 16 * S * 4>(zvo, rws, zso); });
    ld1c(mu, muo, rws, WS_MU);
    u32x4 z[3];
    vmw_bar<6>();
    z[0] = (u32x4){zi[0], zi[32], zi[64], zi[96]};
    z[1] = (u32x4){zi[256], zi[256 + 32], zi[256 + 64], zi[256 + 96]};
    f32x16 acc;
    p3_run<0>(acc, ta, z, zi, w1, xc, mu);
    if (b == 0 && tid == 0) __hip_atomic_store(ctl, tag, RLX_AGENT);
    asm volatile("s_nop 11" : "+v"(acc));
    xw[0] = (f32x4){acc[8], acc[9], acc[10], acc[11]};
    xw[64] = (f32x4){acc[12], acc[13], acc[14], acc[15]};
    bar();
    const f32x4 r0 = xr[0], r1 = xr[64];
    u32x4 h0, h1;
    const float sb = par ? -1.0f : 1.0f;
#pragma unroll
    for (int e = 0; e < 4; ++e) {
      const f32x2 m = {acc[2 * e], acc[2 * e + 1]}, o = half2(e < 2 ? r0 : r1, e & 1);
      const f32x2 a = (m + o) + (f32x2){xc[2 * e], xc[2 * e + 1]}, bb = (m - o) * sb + (f32x2){xc[8 + 2 * e], xc[9 + 2 * e]};
      h0[e] = pack2(a[0], a[1]);
      h1[e] = pack2(bb[0], bb[1]);
    }
    st4c(h0, ho, rws, WS_H + ((m32 >> 1) * 16 + cd) * 4096);
    st4c(h1, ho, rws, WS_H + ((m32 >> 1) * 16 + cd + 8) * 4096);
    float q0 = 0.f, q1 = 0.f;
#pragma unroll
    for (int e = 0; e < 4; ++e) { q0 = dot2(h0[e], h0[e], q0); q1 = dot2(h1[e], h1[e], q1); }
    st1c(f2u(half_sum(q0 + q1)), sto, rws, WS_ST + ((m32 >> 2) * 16 + 2 * cd + par) * 512);
    sfor<52, 56>([&](auto N) { w1_piece<N>(w1); });
    w1_tr_range<8, 11>(w, l);
  }

  u32x4 (&w2v)[NV][4] = c.w2v;
  const u32 w2s = WS_W2 + (cb * 56 + w * 14) * 4096;
  G2 c2;
  const int mq = m64 >> 1, srow = 128 * mq + 64 * (w >> 1) + 32 * (m64 & 1) + 16 * (w & 1) + i;
  c2.vo = l * 16; c2.rws = rws; c2.tag = (u32x2){(u32)tag, (u32)(tag >> 32)};
  c2.fvo = l ? NOWHERE : 0;
  c2.pvo = (l < 14 ? l : 0) * FW * 8; c2.pso = WS_CTL + (4 * 32768 + (mq * 128 + 14 * w) * FW) * 8;
  c2.fl = F4 + (mq * 128 + 14 * w + (l < 14 ? l : 0)) * FW;
  c2.rvo = (((g & 1) * 2) * 16 + i) * 16 + (g >> 1) * 4096; c2.rso = WS_H + (((2 * mq + (w >> 1)) * 16 + 2 * cb) * 4 + 2 * (m64 & 1) + (w & 1)) * 1024;
  c2.mvo = srow * 4;
#pragma unroll
  for (int r = 0; r < 4; ++r) {
    c2.ab[r] = WS_ACT + ((mq * 8 + 4 * (r >> 1) + 2 * (m64 & 1) + (r & 1)) * 56 + 14 * w) * 1024;
    c2.red[r] = (LAS f32x4*)(smem + (r < 3 ? 8192 + r * 16384 : L_SCR)) + w * 256 + l;
  }
  pins(c2.ab[0]); pins(c2.ab[1]); pins(c2.ab[2]); pins(c2.ab[3]); pins(c2.pso); pins(c2.rso);
  pin(c2.pvo); pin(c2.rvo); pin(c2.mvo); pin(c2.red);
  LAS f32x2* lst = (LAS f32x2*)smem;
  c.lst = lst + i;
  c.vo = l * 16;
  c.as = WS_ACT + (m128 * 8 * 56 + 4 * ch + w) * 1024;
  c.la = (const LAS u32x4*)smem + l;
  c.lb = c.la + 4096;
  pin(c.lb);
  c.hs = WS_H + m128 * 131072 + w * 1024; c.hl = w * 1024; c.w2s = w2s; c.rws = rws;
  c.lsw = lst + w * 32 + (l & 31);
  c.w1 = &w1;
  c.f5 = F5 + (cb * 4 + w) * 16; c.f5o = WS_CTL + (5 * 32768 + (cb * 4 + w) * 16) * 8;
  c.stag = ((u64)(u32)__builtin_amdgcn_readfirstlane(c2.tag[1]) << 32) | (u32)__builtin_amdgcn_readfirstlane(c2.tag[0]);
  c.tag = c2.tag; c.fvo = c2.fvo; c.fso = WS_CTL + (4 * 32768 + (m128 * 128 + 4 * ch + w) * FW) * 8;
  c2.fso = ffn ? c.fso + 64 * FW * 8 : NOWHERE;
  w1_tr_addr(w, l, c.tr0, c.tr1);
  c.tr0 += L_SCR; c.tr1 += L_SCR;
  pin(c.tr0); pin(c.tr1);
  u32 spo = ((l >> 5) * 1024 + w * 32 + (l & 31)) * 4;
  pin(c.lst); pin(c.lsw); pin(spo); pin(c.fvo); pin(c.tag);
  pins(c.as); pins(c.fso); pins(c.f5o); pins(c.hs); pins(c.hl); pins(c.w2s); pins(c2.fso);
  publish<4>(F3 + (xcd * 32 + idx) * FS, tag);
  sfor<56, 60>([&](auto N) { w1_piece<N>(w1); });
  if (ffn) {
    w1_tr_range<11, 13>(w, l);
    vmw_bar<4>();
    w1_tr_range<13, 14>(w, l);
    await<1, FS>(w, F3, m128 * 16 + i, l < 16, tag);
    h_piece<0>(c);
    c.hn = c.hs;
    sfor<1, 8>([&](auto K) { dmac_next<4096, 4096>(c.vo, rws, c.hn); });
    sfor<0, 8>([&](auto K) { ld1ci<K * 512>(c.sp[K], spo, rws, WS_ST + m128 * 8192); });
    vmw_bar<12>();
#pragma unroll
    for (int rt = 0; rt < 4; ++rt) c.af[0][rt] = c.la[rt * 64];
    g1_run<0, 0, 3>(c);
    vmw_bar<18>();
    g1_run<0, 3, 7>(c);
    vmw_bar<6>();
    g1_run<0, 7, 15>(c);
    vmw_bar<12>();
    g1_run<0, 15, 16>(c);
    g1_run<1, 0, 16>(c);
    asm volatile("s_nop 7" ::: "memory");
#pragma unroll
    for (int rt = 0; rt < 4; ++rt) {
      pin(c.acc[1][rt]);
      const u32x4 v = glu_tile(c.acc[1][rt], rt ? c.lst[16 * (rt + 4)] : c.e.nx);
      if (rt == 2) ld2c<0>(c2.pf, c2.pvo, rws, c2.pso);
      st4c(v, c.vo, rws, c.as + (4 + rt) * 56 * 1024);
    }
    vmw<2>();
    pin(w2v);
  } else {
    {
      const int j = (b - 224) & 31;
      const srd_t rwd = mk_srd(wd, F * D * 2);
      u32x2 raw[4][8];
      const u32 vo = (4 * g * D + 64 * (j >> 2) + 4 * i) * 2;
      auto rows = [&](int n) {
        const u32 so = (14 * (j & 3) + (w + 4 * n < 14 ? w + 4 * n : 13)) * 32 * D * 2;
        ld2<0>(raw[n][0], vo, rwd, so); ld2<1024>(raw[n][1], vo, rwd, so); ld2<2048>(raw[n][2], vo, rwd, so); ld2<3072>(raw[n][3], vo, rwd, so);
        ld2<0>(raw[n][4], vo, rwd, so + 16384); ld2<1024>(raw[n][5], vo, rwd, so + 16384); ld2<2048>(raw[n][6], vo, rwd, so + 16384); ld2<3072>(raw[n][7], vo, rwd, so + 16384);
      };
      auto frags = [&](int n) {
        pin(raw[n]);
        u32x4 f[4];
        tr8x4(raw[n], f);
        const u32 so = w + 4 * n < 14 ? WS_W2 + (j * 14 + w + 4 * n) * 4096 : NOWHERE;
#pragma unroll
        for (int t = 0; t < 4; ++t) st4c(f[t], l * 16 + t * 1024, rws, so);
      };
      rows(0); rows(1);
      vmw<8>();
      frags(0);
      rows(2); rows(3);
      publish<16>(F5 + j * 16, tag);
      frags(1);
      vmw<12>();
      frags(2);
      vmw<8>();
      frags(3);
      publish<0>(F5 + j * 16 + 8, tag);
      spin_until((u32x2){~c2.tag[0], 0u}, c2.tag, F5 + (cb * 4 + w) * 16 + 8);
    }
#pragma unroll
    for (int k = 0; k < NV; ++k) { const u32 so = w2s + (k) * 4096; ld4c<0>(w2v[k][0], l * 16, rws, so); ld4c<1024>(w2v[k][1], l * 16, rws, so); ld4c<2048>(w2v[k][2], l * 16, rws, so); ld4c<3072>(w2v[k][3], l * 16, rws, so); }
    w2_load_acc_range<NV, 14>(l * 16, rws, w2s);
    ld2c<0>(c2.pf, c2.pvo, rws, c2.pso);
    vmw<0>();
    pin(w2v);
  }

  {
    pin(c2.pf);
    spin_until(c2.pf, c2.tag, c2.fl);
    sfor<0, WIN>([&](auto N) { g2_load<N>(c2); });
    g2_run<0>(c2, w2v);
    NOP_OUT4(c2.acc[3][0], c2.acc[3][1], c2.acc[3][2], c2.acc[3][3]);
#pragma unroll
    for (int q = 0; q < 4; ++q) c2.red[3][q * 64] = c2.acc[3][q];
    const LAS f32x4* red = (const LAS f32x4*)(smem + (w < 3 ? 8192 + w * 16384 : L_SCR)) + l;
    GAS u32x4* op = (GAS u32x4*)(out + srow * D + 64 * cb + 16 * g);
    pin(red); pin(op);
    bar();
    f32x4 pr[16], o[4];
#pragma unroll
    for (int k = 0; k < 16; ++k) pr[k] = red[(k & 3) * 256 + (k >> 2) * 64];
    SB();
#pragma unroll
    for (int q = 0; q < 4; ++q)
      o[q] = ((pr[4 * q] + (f32x4){c2.rr[q], c2.rr[4 + q], c2.rr[8 + q], c2.rr[12 + q]}) + pr[4 * q + 1]) + (pr[4 * q + 2] + pr[4 * q + 3]);
    u32x4 v0, v1;
#pragma unroll
    for (int e = 0; e < 4; ++e) {
      v0[e] = pack2(o[(2 * e) & 3][(2 * e) >> 2], o[(2 * e + 1) & 3][(2 * e + 1) >> 2]);
      v1[e] = pack2(o[(2 * e) & 3][2 + ((2 * e) >> 2)], o[(2 * e + 1) & 3][2 + ((2 * e + 1) >> 2)]);
    }
    op[0] = v0;
    op[1] = v1;
  }
}

__global__ __launch_bounds__(256, 1) void fused(const u16* __restrict__ x, const u16* __restrict__ wr, const u16* __restrict__ wi,
                                                const u16* __restrict__ wg, const u16* __restrict__ wu, const u16* __restrict__ wd,
                                                unsigned char* __restrict__ ws, u16* __restrict__ out) {
  u64 p;
  {
    asm volatile("s_getpc_b64 %0" : "=s"(p));
    const u32 o = min((((blockIdx.x >> 3) & 3) * 68 + (threadIdx.x >> 6) * 17 + min(threadIdx.x & 63u, 16u)) * 128u, (u32)CODE_BYTES);
    asm volatile("global_load_dword a255, %0, %1" :: "v"(o), "s"(p) : "memory", ACLOB);
    SB();
  }
  body(x, wr, wi, wg, wu, wd, ws, out);
}

extern "C" void kernel_launch(void* const* d_in, const int* in_sizes, int n_in, void* d_out, int out_size, void* d_ws, size_t ws_size,
                              hipStream_t stream) {
  (void)hipFuncSetAttribute((const void*)fused, hipFuncAttributeMaxDynamicSharedMemorySize, LDS_BYTES);
  fused<<<256, 256, LDS_BYTES, stream>>>((const u16*)d_in[0], (const u16*)d_in[1], (const u16*)d_in[2], (const u16*)d_in[4], (const u16*)d_in[5],
                                         (const u16*)d_in[6], (unsigned char*)d_ws, (u16*)d_out);
}
```
